```python
import torch

def _softmax(x, axis=-1):
    return torch.softmax(x, dim=axis)
def _gelu(x):
    return torch.nn.functional.gelu(x, approximate="tanh")
def _silu(x):
    return torch.nn.functional.silu(x)
def _sigmoid(x):
    return torch.sigmoid(x)
def _layernorm(x, axis=-1, eps=1e-5):
    m = x.mean(dim=axis, keepdim=True)
    v = x.var(dim=axis, keepdim=True, unbiased=False)
    return (x - m) / torch.sqrt(v + eps)
def _scatter_add(tgt, idx, src, axis=0):
    return tgt.clone().index_add_(axis, idx, src)
def _segment_sum(src, seg, n):
    out = torch.zeros((n,) + src.shape[1:], dtype=src.dtype)
    return out.index_add_(0, seg, src)
def _pad(x, pw):
    flat = tuple(v for pair in reversed(pw) for v in pair)
    return torch.nn.functional.pad(x, flat)

kernel_name = 'interleave_layers__mha_block__glu_gated_glu__prompt'

def setup_inputs():

    _w = (lambda a: (lambda t: t.to(torch.bfloat16) if t.is_floating_point() else t)(torch.as_tensor(a)))
    def _A_setup():
        B, S, D, H = 1, 2048, 448, 16
        return {
            "x": _w((lambda *s: torch.randn(*s, dtype=torch.bfloat16) * 0.05)(B, S, D)),
            "wqkv": _w((lambda *s: torch.randn(*s, dtype=torch.bfloat16) * 0.05)(D, 3 * D)),
            "wo": _w((lambda *s: torch.randn(*s, dtype=torch.bfloat16) * 0.05)(D, D)),
        }
    _A_in = _A_setup()

    def _B_setup():
        B, S, D, F = 1, 2048, 448, 1792
        return {
            "x": _w((lambda *s: torch.randn(*s, dtype=torch.bfloat16) * 0.05)(B, S, D)),
            "w_gate": _w((lambda *s: torch.randn(*s, dtype=torch.bfloat16) * 0.05)(D, F)),
            "w_up": _w((lambda *s: torch.randn(*s, dtype=torch.bfloat16) * 0.05)(D, F)),
            "w_down": _w((lambda *s: torch.randn(*s, dtype=torch.bfloat16) * 0.05)(F, D)),
        }
    _B_in = _B_setup()

    return {
        "A_x": _A_in["x"],
        "A_wqkv": _A_in["wqkv"],
        "A_wo": _A_in["wo"],
        "B_x": _B_in["x"],
        "B_w_gate": _B_in["w_gate"],
        "B_w_up": _B_in["w_up"],
        "B_w_down": _B_in["w_down"],
    }

def reference(A_x, A_wqkv, A_wo, B_x, B_w_gate, B_w_up, B_w_down):
    def _A_ref(x, wqkv, wo):
        B, S, D = x.shape
        H = 16
        Dh = D // H
        qkv = (lambda x, *d: x.permute(*d))((x @ wqkv).reshape(B, S, 3, H, Dh), 2, 0, 3, 1, 4)
        q, k, v = qkv[0], qkv[1], qkv[2]
        scores = torch.einsum("bhqd,bhkd->bhqk", q, k) * (Dh ** -0.5)
        p = _softmax(scores, axis=-1)
        o = torch.einsum("bhqk,bhkd->bhqd", p, v)
        return (lambda x, *d: x.permute(*d))(o, 0, 2, 1, 3).reshape(B, S, D) @ wo

    def _B_ref(x, w_gate, w_up, w_down):
        g = torch.einsum("bsd,df->bsf", x, w_gate)
        u = torch.einsum("bsd,df->bsf", x, w_up)
        h = _sigmoid(g) * u
        return torch.einsum("bsf,fd->bsd", h, w_down)

    x = A_x
    x = x + _A_ref(_layernorm(x, axis=-1), A_wqkv, A_wo)
    x = x + _B_ref(_layernorm(x, axis=-1), B_w_gate, B_w_up, B_w_down)
    x = x + _A_ref(_layernorm(x, axis=-1), A_wqkv, A_wo)
    x = x + _B_ref(_layernorm(x, axis=-1), B_w_gate, B_w_up, B_w_down)
    return x
```

```cpp
#include <hip/hip_runtime.h>
#include <hip/hip_bf16.h>
#include <stdint.h>

typedef unsigned short u16;
using bf16x8 = __attribute__((ext_vector_type(8))) short;
using s16x4  = __attribute__((ext_vector_type(4))) short;
using f32x16 = __attribute__((ext_vector_type(16))) float;
using u32x4  = __attribute__((ext_vector_type(4))) unsigned;
using u32x2  = __attribute__((ext_vector_type(2))) unsigned;

#define DI __device__ __forceinline__
#define MFMA32(a, b, c) __builtin_amdgcn_mfma_f32_32x32x16_bf16((a), (b), (c), 0, 0, 0)

constexpr int SEQ = 2048, DM = 448, NH = 16, DH = 28, FF = 1792;

DI float bf2f(u16 v) { return __uint_as_float(((unsigned)v) << 16); }
typedef __bf16 bf16x2_t __attribute__((ext_vector_type(2)));
typedef float f32x2_t __attribute__((ext_vector_type(2)));
DI unsigned cvtpk(float lo, float hi) { f32x2_t v = {lo, hi}; return __builtin_bit_cast(unsigned, __builtin_convertvector(v, bf16x2_t)); }
DI u16 f2bf(float x) { return (u16)(cvtpk(x, x) & 0xffffu); }
DI float rbf(float x) { return bf2f(f2bf(x)); }

typedef short v4i16_t __attribute__((ext_vector_type(4)));
DI s16x4 tr_read(const u16* p) {
  return __builtin_bit_cast(s16x4, __builtin_amdgcn_ds_read_tr16_b64_v4i16((__attribute__((address_space(3))) v4i16_t*)p));
}

template <int BM, int MODE>
__global__ __launch_bounds__(256) void gemm_kernel(const u16* __restrict__ A, int K, const u16* __restrict__ W0,
                                                   const u16* __restrict__ W1, int N, const u16* __restrict__ resid,
                                                   u16* __restrict__ out, u16* __restrict__ Qb, u16* __restrict__ Kb,
                                                   u16* __restrict__ Vt) {
  constexpr int NB = (MODE == 2) ? 2 : 1;
  constexpr int BSTR = 96;
  __shared__ __attribute__((aligned(16))) u16 sA[2][BM * 64];
  __shared__ __attribute__((aligned(16))) u16 sB[2][NB][64 * BSTR];
  __shared__ float sStat[BM][2];

  const int tid = threadIdx.x, w = tid >> 6, lane = tid & 63, r = lane & 31, hh = lane >> 5;
  const int n0 = blockIdx.x * 64, m0 = blockIdx.y * BM;
  const int wm = w >> 1, wn = w & 1;

  if (MODE != 1) {
    for (int row = w; row < BM; row += 4) {
      float v[8]; float s = 0.f;
      if (lane < 56) {
        uint4 t = *reinterpret_cast<const uint4*>(A + (size_t)(m0 + row) * K + lane * 8);
        unsigned u[4] = {t.x, t.y, t.z, t.w};
        for (int j = 0; j < 4; ++j) { v[2 * j] = __uint_as_float(u[j] << 16); v[2 * j + 1] = __uint_as_float(u[j] & 0xffff0000u); }
        for (int j = 0; j < 8; ++j) s += v[j];
      } else { for (int j = 0; j < 8; ++j) v[j] = 0.f; }
      for (int o = 32; o > 0; o >>= 1) s += __shfl_xor(s, o);
      float mean = s * (1.f / 448.f);
      float q = 0.f;
      if (lane < 56) for (int j = 0; j < 8; ++j) { float d = v[j] - mean; q += d * d; }
      for (int o = 32; o > 0; o >>= 1) q += __shfl_xor(q, o);
      float var = q * (1.f / 448.f);
      if (lane == 0) {
        sStat[row][0] = rbf(mean);
        sStat[row][1] = rbf(sqrtf(rbf(rbf(var) + 1e-5f)));
      }
    }
    __syncthreads();
  }

  constexpr int AIT = BM / 32;
  u32x4 ra[AIT]; u32x4 rb[NB][2];
  auto gload = [&](int k0) {
#pragma unroll
    for (int i = 0; i < AIT; ++i) {
      int row = (tid >> 3) + 32 * i, c = tid & 7;
      ra[i] = *reinterpret_cast<const u32x4*>(A + (size_t)(m0 + row) * K + k0 + c * 8);
    }
#pragma unroll
    for (int b = 0; b < NB; ++b) {
      const u16* Wp = b ? W1 : W0;
#pragma unroll
      for (int i = 0; i < 2; ++i) {
        int kr = (tid >> 3) + 32 * i, c = tid & 7;
        rb[b][i] = *reinterpret_cast<const u32x4*>(Wp + (size_t)(k0 + kr) * N + n0 + c * 8);
      }
    }
  };
  auto swrite = [&](int buf) {
#pragma unroll
    for (int i = 0; i < AIT; ++i) {
      int row = (tid >> 3) + 32 * i, c = tid & 7;
      u32x4 t = ra[i];
      if (MODE != 1) {
        float mean = sStat[row][0], den = sStat[row][1];
        #pragma unroll
        for (int j = 0; j < 4; ++j) {
          float a = __uint_as_float(t[j] << 16), b = __uint_as_float(t[j] & 0xffff0000u);
          a = rbf(a - mean) / den; b = rbf(b - mean) / den;
          t[j] = cvtpk(a, b);
        }
      }
      *reinterpret_cast<u32x4*>(&sA[buf][row * 64 + ((c ^ ((row >> 1) & 7)) * 8)]) = t;
    }
#pragma unroll
    for (int b = 0; b < NB; ++b)
#pragma unroll
      for (int i = 0; i < 2; ++i) {
        int kr = (tid >> 3) + 32 * i, c = tid & 7;
        *reinterpret_cast<u32x4*>(&sB[buf][b][kr * BSTR + c * 8]) = rb[b][i];
      }
  };

  constexpr int MT = BM / 64;
  f32x16 acc[NB][MT];
#pragma unroll
  for (int b = 0; b < NB; ++b)
#pragma unroll
    for (int mt = 0; mt < MT; ++mt)
#pragma unroll
      for (int i = 0; i < 16; ++i) acc[b][mt][i] = 0.f;

  const int nk = K / 64;
  gload(0); swrite(0); __syncthreads();
  for (int kt = 0; kt < nk; ++kt) {
    const int buf = kt & 1;
    if (kt + 1 < nk) gload((kt + 1) * 64);
#pragma unroll
    for (int ks = 0; ks < 4; ++ks) {
      bf16x8 af[MT];
#pragma unroll
      for (int mt = 0; mt < MT; ++mt) {
        int row = wm * (BM / 2) + mt * 32 + r;
        int c = ks * 2 + hh;
        af[mt] = *reinterpret_cast<const bf16x8*>(&sA[buf][row * 64 + ((c ^ ((row >> 1) & 7)) * 8)]);
      }
#pragma unroll
      for (int b = 0; b < NB; ++b) {
        int G = lane >> 4, i16 = lane & 15;
        int col0 = wn * 32 + (G & 1) * 16, kb = ks * 16 + 8 * (G >> 1);
        const u16* p = &sB[buf][b][(kb + (i16 >> 2)) * BSTR + col0 + 4 * (i16 & 3)];
        s16x4 lo = tr_read(p), hi = tr_read(p + 4 * BSTR);
        bf16x8 bfrag = __builtin_shufflevector(lo, hi, 0, 1, 2, 3, 4, 5, 6, 7);
#pragma unroll
        for (int mt = 0; mt < MT; ++mt) acc[b][mt] = MFMA32(bfrag, af[mt], acc[b][mt]);
      }
    }
    if (kt + 1 < nk) { swrite(buf ^ 1); }
    __syncthreads();
  }

#pragma unroll
  for (int mt = 0; mt < MT; ++mt) {
    const int m = m0 + wm * (BM / 2) + mt * 32 + r;
#pragma unroll
    for (int g = 0; g < 4; ++g) {
      const int n = n0 + wn * 32 + 8 * g + 4 * hh;
      if (MODE == 1) {
        u32x2 rv = *reinterpret_cast<const u32x2*>(resid + (size_t)m * N + n);
        float x0 = __uint_as_float(rv[0] << 16), x1 = __uint_as_float(rv[0] & 0xffff0000u);
        float x2 = __uint_as_float(rv[1] << 16), x3 = __uint_as_float(rv[1] & 0xffff0000u);
        u32x2 o;
        o[0] = cvtpk(x0 + rbf(acc[0][mt][4 * g + 0]), x1 + rbf(acc[0][mt][4 * g + 1]));
        o[1] = cvtpk(x2 + rbf(acc[0][mt][4 * g + 2]), x3 + rbf(acc[0][mt][4 * g + 3]));
        *reinterpret_cast<u32x2*>(out + (size_t)m * N + n) = o;
      } else if (MODE == 2) {
        float hv[4];
#pragma unroll
        for (int j = 0; j < 4; ++j) {
          float gv = rbf(acc[0][mt][4 * g + j]), uv = rbf(acc[1][mt][4 * g + j]);
          float sg = rbf(1.f / (1.f + __expf(-gv)));
          hv[j] = sg * uv;
        }
        u32x2 o; o[0] = cvtpk(hv[0], hv[1]); o[1] = cvtpk(hv[2], hv[3]);
        *reinterpret_cast<u32x2*>(out + (size_t)m * N + n) = o;
      } else {
        const int which = n / DM, rem = n % DM, head = rem / DH, d = rem % DH;
        if (which == 0) {
          const float c = 0.18898223650461362f * 1.4426950408889634f;
          u32x2 o; o[0] = cvtpk(acc[0][mt][4 * g] * c, acc[0][mt][4 * g + 1] * c); o[1] = cvtpk(acc[0][mt][4 * g + 2] * c, acc[0][mt][4 * g + 3] * c);
          u16* p = Qb + ((size_t)head * SEQ + m) * 32 + d;
          *reinterpret_cast<u32x2*>(p) = o;
          if (d == 24) { u32x2 z = {0u, 0u}; *reinterpret_cast<u32x2*>(p + 4) = z; }
        } else if (which == 1) {
          u32x2 o; o[0] = cvtpk(acc[0][mt][4 * g], acc[0][mt][4 * g + 1]); o[1] = cvtpk(acc[0][mt][4 * g + 2], acc[0][mt][4 * g + 3]);
          u16* p = Kb + ((size_t)head * SEQ + m) * 32 + d;
          *reinterpret_cast<u32x2*>(p) = o;
          if (d == 24) { u32x2 z = {0u, 0u}; *reinterpret_cast<u32x2*>(p + 4) = z; }
        } else {
          const int t = m & 15;
          const int pos = (m & ~15) + ((t >= 4 && t < 8) ? t + 4 : ((t >= 8 && t < 12) ? t - 4 : t));
          u16* p = Vt + ((size_t)head * 32 + d) * SEQ + pos;
#pragma unroll
          for (int j = 0; j < 4; ++j) p[(size_t)j * SEQ] = f2bf(acc[0][mt][4 * g + j]);
          if (d == 24) { p[(size_t)4 * SEQ] = 0x3f80; p[(size_t)5 * SEQ] = 0; p[(size_t)6 * SEQ] = 0; p[(size_t)7 * SEQ] = 0; }
        }
      }
    }
  }
}

__global__ __launch_bounds__(256) void attn_kernel(const u16* __restrict__ Qb, const u16* __restrict__ Kb,
                                                   const u16* __restrict__ Vt, u16* __restrict__ o) {
  __shared__ __attribute__((aligned(16))) u16 sK[2][128 * 32];
  __shared__ __attribute__((aligned(16))) u16 sV[2][32 * 128];
  const int tid = threadIdx.x, w = tid >> 6, lane = tid & 63, r = lane & 31, hh = lane >> 5;
  const int head = blockIdx.x >> 4, qblk = blockIdx.x & 15;
  const int q0 = qblk * 128 + w * 32;
  const u16* Kh = Kb + (size_t)head * SEQ * 32;
  const u16* Vh = Vt + (size_t)head * 32 * SEQ;

  bf16x8 qf[2];
#pragma unroll
  for (int ks = 0; ks < 2; ++ks)
    qf[ks] = *reinterpret_cast<const bf16x8*>(Qb + ((size_t)head * SEQ + q0 + r) * 32 + ks * 16 + 8 * hh);

  u32x4 rk[2], rv[2];
  auto gload = [&](int key0) {
#pragma unroll
    for (int i = 0; i < 2; ++i) {
      int ch = tid + 256 * i;
      rk[i] = *reinterpret_cast<const u32x4*>(Kh + (size_t)key0 * 32 + ch * 8);
      int d = ch >> 4, c = ch & 15;
      rv[i] = *reinterpret_cast<const u32x4*>(Vh + (size_t)d * SEQ + key0 + c * 8);
    }
  };
  auto swrite = [&](int buf) {
#pragma unroll
    for (int i = 0; i < 2; ++i) {
      int ch = tid + 256 * i;
      int row = ch >> 2, c = ch & 3;
      *reinterpret_cast<u32x4*>(&sK[buf][row * 32 + ((c ^ ((row >> 2) & 3)) * 8)]) = rk[i];
      int d = ch >> 4, cv = ch & 15;
      *reinterpret_cast<u32x4*>(&sV[buf][d * 128 + ((cv ^ (d & 15)) * 8)]) = rv[i];
    }
  };

  f32x16 oacc, negm;
#pragma unroll
  for (int i = 0; i < 16; ++i) { oacc[i] = 0.f; negm[i] = 0.f; }
  bool first = true;

  gload(0); swrite(0); __syncthreads();
  for (int st = 0; st < SEQ / 128; ++st) {
    const int buf = st & 1;
    if (st + 1 < SEQ / 128) gload((st + 1) * 128);
#pragma unroll
    for (int sub = 0; sub < 4; ++sub) {
      bf16x8 kf[2], vf[2];
#pragma unroll
      for (int ks = 0; ks < 2; ++ks) {
        int row = sub * 32 + r, c = ks * 2 + hh;
        kf[ks] = *reinterpret_cast<const bf16x8*>(&sK[buf][row * 32 + ((c ^ ((row >> 2) & 3)) * 8)]);
        int cv = sub * 4 + ks * 2 + hh;
        vf[ks] = *reinterpret_cast<const bf16x8*>(&sV[buf][r * 128 + ((cv ^ (r & 15)) * 8)]);
      }
      f32x16 s = MFMA32(kf[0], qf[0], negm);
      s = MFMA32(kf[1], qf[1], s);
      float tmax = s[0];
#pragma unroll
      for (int i = 1; i < 16; ++i) tmax = fmaxf(tmax, s[i]);
      if (first || __any(tmax > 8.f)) {
        float other = __shfl_xor(tmax, 32);
        float tm = fmaxf(tmax, other);
        float delta = first ? tm : fmaxf(tm, 0.f);
        float f = __builtin_amdgcn_exp2f(-delta);
#pragma unroll
        for (int i = 0; i < 16; ++i) { s[i] -= delta; negm[i] -= delta; oacc[i] *= f; }
        first = false;
      }
      u32x4 p0, p1;
#pragma unroll
      for (int j = 0; j < 4; ++j) {
        p0[j] = cvtpk(__builtin_amdgcn_exp2f(s[2 * j]), __builtin_amdgcn_exp2f(s[2 * j + 1]));
        p1[j] = cvtpk(__builtin_amdgcn_exp2f(s[8 + 2 * j]), __builtin_amdgcn_exp2f(s[8 + 2 * j + 1]));
      }
      oacc = MFMA32(vf[0], __builtin_bit_cast(bf16x8, p0), oacc);
      oacc = MFMA32(vf[1], __builtin_bit_cast(bf16x8, p1), oacc);
    }
    if (st + 1 < SEQ / 128) swrite(buf ^ 1);
    __syncthreads();
  }
  float l = __shfl(oacc[12], r + 32);
  float inv = 1.f / l;
  u16* op = o + (size_t)(q0 + r) * DM + head * DH;
#pragma unroll
  for (int g = 0; g < 4; ++g) {
    int d = 8 * g + 4 * hh;
    if (d < DH) {
      u32x2 v; v[0] = cvtpk(oacc[4 * g] * inv, oacc[4 * g + 1] * inv); v[1] = cvtpk(oacc[4 * g + 2] * inv, oacc[4 * g + 3] * inv);
      *reinterpret_cast<u32x2*>(op + d) = v;
    }
  }
}

extern "C" void kernel_launch(void* const* d_in, const int* in_sizes, int n_in,
                              void* d_out, int out_size, void* d_ws, size_t ws_size,
                              hipStream_t stream) {
  const u16* x0 = (const u16*)d_in[0];
  const u16* wqkv = (const u16*)d_in[1];
  const u16* wo = (const u16*)d_in[2];
  const u16* wg = (const u16*)d_in[4];
  const u16* wu = (const u16*)d_in[5];
  const u16* wd = (const u16*)d_in[6];
  u16* out = (u16*)d_out;
  char* ws = (char*)d_ws;
  size_t off = 0;
  auto take = [&](size_t bytes) { void* p = ws + off; off += (bytes + 255) & ~(size_t)255; return p; };
  u16* Qb = (u16*)take((size_t)NH * SEQ * 32 * 2);
  u16* Kb = (u16*)take((size_t)NH * SEQ * 32 * 2);
  u16* Vt = (u16*)take((size_t)NH * SEQ * 32 * 2);
  u16* ob = (u16*)take((size_t)SEQ * DM * 2);
  u16* x1 = (u16*)take((size_t)SEQ * DM * 2);
  u16* x2 = (u16*)take((size_t)SEQ * DM * 2);
  u16* hb = (u16*)take((size_t)SEQ * FF * 2);

  const u16* xin = x0;
  for (int layer = 0; layer < 2; ++layer) {
    u16* xo = (layer == 0) ? x2 : out;
    gemm_kernel<128, 0><<<dim3(21, 16), 256, 0, stream>>>(xin, DM, wqkv, nullptr, 3 * DM, nullptr, nullptr, Qb, Kb, Vt);
    attn_kernel<<<256, 256, 0, stream>>>(Qb, Kb, Vt, ob);
    gemm_kernel<64, 1><<<dim3(7, 32), 256, 0, stream>>>(ob, DM, wo, nullptr, DM, xin, x1, nullptr, nullptr, nullptr);
    gemm_kernel<128, 2><<<dim3(28, 16), 256, 0, stream>>>(x1, DM, wg, wu, FF, nullptr, hb, nullptr, nullptr, nullptr);
    gemm_kernel<64, 1><<<dim3(7, 32), 256, 0, stream>>>(hb, FF, wd, nullptr, DM, x1, xo, nullptr, nullptr, nullptr);
    xin = xo;
  }
}
```

```cpp
#include <hip/hip_runtime.h>
#include <hip/hip_bf16.h>
#include <stdint.h>

typedef unsigned short u16;
using bf16x8 = __attribute__((ext_vector_type(8))) short;
using s16x4  = __attribute__((ext_vector_type(4))) short;
using f32x16 = __attribute__((ext_vector_type(16))) float;
using u32x4  = __attribute__((ext_vector_type(4))) unsigned;
using u32x2  = __attribute__((ext_vector_type(2))) unsigned;

#define DI __device__ __forceinline__
#define MFMA32(a, b, c) __builtin_amdgcn_mfma_f32_32x32x16_bf16((a), (b), (c), 0, 0, 0)

constexpr int SEQ = 2048, DM = 448, NH = 16, DH = 28, FF = 1792;

DI float bf2f(u16 v) { return __uint_as_float(((unsigned)v) << 16); }
typedef __bf16 bf16x2_t __attribute__((ext_vector_type(2)));
typedef float f32x2_t __attribute__((ext_vector_type(2)));
DI unsigned cvtpk(float lo, float hi) { f32x2_t v = {lo, hi}; return __builtin_bit_cast(unsigned, __builtin_convertvector(v, bf16x2_t)); }
DI u16 f2bf(float x) { return (u16)(cvtpk(x, x) & 0xffffu); }
DI float rbf(float x) { return bf2f(f2bf(x)); }

typedef short v4i16_t __attribute__((ext_vector_type(4)));
DI s16x4 tr_read(const u16* p) {
  return __builtin_bit_cast(s16x4, __builtin_amdgcn_ds_read_tr16_b64_v4i16((__attribute__((address_space(3))) v4i16_t*)p));
}

__global__ __launch_bounds__(1024) void prep_kernel(const u16* __restrict__ x, const u16* __restrict__ wqkv,
                                                    const u16* __restrict__ wg, const u16* __restrict__ wu,
                                                    float* __restrict__ csum, float2* __restrict__ stat0) {
  __shared__ float red[16][64];
  const int tid = threadIdx.x, lane = tid & 63, w = tid >> 6;
  if (blockIdx.x < 77) {
    const int cb = blockIdx.x * 64;
    const u16* W; int N, c0;
    if (cb < 1344) { W = wqkv; N = 1344; c0 = cb; }
    else if (cb < 3136) { W = wg; N = FF; c0 = cb - 1344; }
    else { W = wu; N = FF; c0 = cb - 3136; }
    float s = 0.f;
#pragma unroll
    for (int i = 0; i < 28; ++i) s += bf2f(W[(size_t)(w * 28 + i) * N + c0 + lane]);
    red[w][lane] = s;
    __syncthreads();
    if (w == 0) {
      float t = 0.f;
#pragma unroll
      for (int i = 0; i < 16; ++i) t += red[i][lane];
      csum[cb + lane] = t;
    }
  } else {
    const int row = (blockIdx.x - 77) * 16 + w;
    float v[8]; float s = 0.f;
    if (lane < 56) {
      u32x4 t = *reinterpret_cast<const u32x4*>(x + (size_t)row * DM + lane * 8);
#pragma unroll
      for (int j = 0; j < 4; ++j) { v[2 * j] = __uint_as_float(t[j] << 16); v[2 * j + 1] = __uint_as_float(t[j] & 0xffff0000u); }
#pragma unroll
      for (int j = 0; j < 8; ++j) s += v[j];
    } else {
#pragma unroll
      for (int j = 0; j < 8; ++j) v[j] = 0.f;
    }
    for (int o = 32; o > 0; o >>= 1) s += __shfl_xor(s, o);
    const float mean = s * (1.f / 448.f);
    float q = 0.f;
    if (lane < 56) {
#pragma unroll
      for (int j = 0; j < 8; ++j) { float d = v[j] - mean; q += d * d; }
    }
    for (int o = 32; o > 0; o >>= 1) q += __shfl_xor(q, o);
    if (lane == 0) stat0[row] = make_float2(rbf(mean), 1.f / rbf(sqrtf(rbf(rbf(q * (1.f / 448.f)) + 1e-5f))));
  }
}

DI void glds16(const void* g, unsigned lds_base) {
  unsigned sv;
  asm volatile("s_mov_b32 %0, m0\n\ts_mov_b32 m0, %2\n\ts_nop 0\n\tglobal_load_lds_dwordx4 %1, off\n\ts_mov_b32 m0, %0"
               : "=&s"(sv) : "v"(g), "s"(lds_base) : "memory");
}
#define GLDS(gp, lp) glds16((gp), (unsigned)__builtin_amdgcn_readfirstlane((unsigned)(uintptr_t)(lp)))

template <int BM, int MODE, int NST>
__global__ __launch_bounds__(256) void gemm_kernel(const u16* __restrict__ A, int K, const u16* __restrict__ W0,
                                                   const u16* __restrict__ W1, int N, const u16* __restrict__ resid,
                                                   u16* __restrict__ out, u16* __restrict__ Qb, u16* __restrict__ Kb,
                                                   u16* __restrict__ Vt, const float* __restrict__ csum,
                                                   const float2* __restrict__ stat_in, int nstat, float2* __restrict__ stat_out) {
  constexpr int NB = (MODE == 2) ? 2 : 1;
  constexpr int ASZ = BM * 64, BSZ = 64 * 64;
  __shared__ __attribute__((aligned(16))) u16 smem[NST * (ASZ + NB * BSZ)];
  constexpr int GPS = BM / 32 + 2 * NB;

  const int tid = threadIdx.x, w = tid >> 6, lane = tid & 63, r = lane & 31, hh = lane >> 5;
  const int n0 = blockIdx.x * 64, m0 = blockIdx.y * BM;
  const int wm = w >> 1, wn = w & 1;

  auto stage = [&](int buf, int k0) {
    u16* sa = smem + buf * (ASZ + NB * BSZ);
#pragma unroll
    for (int i = 0; i < BM / 32; ++i) {
      const int ch = i * 256 + tid, row = ch >> 3, c = ch & 7;
      GLDS(A + (size_t)(m0 + row) * K + k0 + ((c ^ ((row >> 1) & 7)) * 8), sa + (i * 256 + (tid & ~63)) * 8);
    }
#pragma unroll
    for (int b = 0; b < NB; ++b) {
      const u16* Wp = b ? W1 : W0;
#pragma unroll
      for (int i = 0; i < 2; ++i) {
        const int ch = i * 256 + tid, kr = ch >> 3, c = ch & 7;
        GLDS(Wp + (size_t)(k0 + kr) * N + n0 + ((c ^ (((kr >> 1) & 1) << 2)) * 8), sa + ASZ + b * BSZ + (i * 256 + (tid & ~63)) * 8);
      }
    }
  };

  constexpr int MT = BM / 64;
  f32x16 acc[NB][MT];
#pragma unroll
  for (int b = 0; b < NB; ++b)
#pragma unroll
    for (int mt = 0; mt < MT; ++mt)
#pragma unroll
      for (int i = 0; i < 16; ++i) acc[b][mt][i] = 0.f;

  const int nk = K / 64;
#pragma unroll
  for (int st = 0; st < NST - 1; ++st) if (st < nk) stage(st, st * 64);
  int rbuf = 0, wbuf = NST - 1;
  for (int kt = 0; kt < nk; ++kt) {
    if (kt + NST - 1 < nk) { asm volatile("s_waitcnt vmcnt(%0)" :: "n"(GPS * (NST - 2)) : "memory"); }
    else { asm volatile("s_waitcnt vmcnt(0)" ::: "memory"); }
    __builtin_amdgcn_s_barrier();
    if (kt + NST - 1 < nk) stage(wbuf, (kt + NST - 1) * 64);
    const int buf = rbuf;
    rbuf = (rbuf + 1 == NST) ? 0 : rbuf + 1; wbuf = (wbuf + 1 == NST) ? 0 : wbuf + 1;
    const u16* sa = smem + buf * (ASZ + NB * BSZ);
#pragma unroll
    for (int ks = 0; ks < 4; ++ks) {
      bf16x8 af[MT];
#pragma unroll
      for (int mt = 0; mt < MT; ++mt) {
        const int row = wm * (BM / 2) + mt * 32 + r, c = ks * 2 + hh;
        af[mt] = *reinterpret_cast<const bf16x8*>(&sa[row * 64 + ((c ^ ((row >> 1) & 7)) * 8)]);
      }
#pragma unroll
      for (int b = 0; b < NB; ++b) {
        const int G = lane >> 4, i16 = lane & 15;
        const int col0 = wn * 32 + (G & 1) * 16 + 4 * (i16 & 3), kb = ks * 16 + 8 * (G >> 1) + (i16 >> 2);
        const u16* sb = sa + ASZ + b * BSZ;
        const int ck = col0 >> 3, sub = col0 & 7;
        s16x4 lo = tr_read(&sb[kb * 64 + ((ck ^ (((kb >> 1) & 1) << 2)) * 8) + sub]);
        s16x4 hi = tr_read(&sb[(kb + 4) * 64 + ((ck ^ ((((kb + 4) >> 1) & 1) << 2)) * 8) + sub]);
        bf16x8 bfrag = __builtin_shufflevector(lo, hi, 0, 1, 2, 3, 4, 5, 6, 7);
#pragma unroll
        for (int mt = 0; mt < MT; ++mt) acc[b][mt] = MFMA32(bfrag, af[mt], acc[b][mt]);
      }
    }
  }

  __syncthreads();
  u16* T = smem;
  const bool vtile = (MODE == 0) && (n0 >= 2 * DM);
#pragma unroll
  for (int mt = 0; mt < MT; ++mt) {
    const int lr = wm * (BM / 2) + mt * 32 + r, m = m0 + lr;
    float mean = 0.f, rden = 1.f;
    if (MODE != 1) {
      if (nstat == 1) { float2 st = stat_in[m]; mean = st.x; rden = st.y; }
      else {
        float s1 = 0.f, s2 = 0.f;
        for (int t = 0; t < nstat; ++t) { float2 st = stat_in[(size_t)m * nstat + t]; s1 += st.x; s2 += st.y; }
        const float mu = s1 * (1.f / 448.f);
        const float var = fmaxf(s2 * (1.f / 448.f) - mu * mu, 0.f);
        mean = rbf(mu); rden = 1.f / rbf(sqrtf(rbf(rbf(var) + 1e-5f)));
      }
    }
#pragma unroll
    for (int g = 0; g < 4; ++g) {
      const int lc = wn * 32 + 8 * g + 4 * hh, n = n0 + lc;
      float v[4];
      if (MODE == 1) {
#pragma unroll
        for (int j = 0; j < 4; ++j) v[j] = acc[0][mt][4 * g + j];
      } else if (MODE == 2) {
        float4 cg = *reinterpret_cast<const float4*>(csum + 1344 + n), cu = *reinterpret_cast<const float4*>(csum + 3136 + n);
        const float cgv[4] = {cg.x, cg.y, cg.z, cg.w}, cuv[4] = {cu.x, cu.y, cu.z, cu.w};
#pragma unroll
        for (int j = 0; j < 4; ++j) {
          float gv = rbf((acc[0][mt][4 * g + j] - mean * cgv[j]) * rden), uv = rbf((acc[1][mt][4 * g + j] - mean * cuv[j]) * rden);
          float sg = rbf(1.f / (1.f + __expf(-gv)));
          v[j] = sg * uv;
        }
      } else {
        float4 cs = *reinterpret_cast<const float4*>(csum + n);
        const float c = (n0 < DM) ? 0.18898223650461362f * 1.4426950408889634f : 1.f;
        v[0] = (acc[0][mt][4 * g + 0] - mean * cs.x) * (rden * c); v[1] = (acc[0][mt][4 * g + 1] - mean * cs.y) * (rden * c);
        v[2] = (acc[0][mt][4 * g + 2] - mean * cs.z) * (rden * c); v[3] = (acc[0][mt][4 * g + 3] - mean * cs.w) * (rden * c);
      }
      if (vtile) {
#pragma unroll
        for (int j = 0; j < 4; ++j) T[(lc + j) * BM + lr] = f2bf(v[j]);
      } else {
        u32x2 o; o[0] = cvtpk(v[0], v[1]); o[1] = cvtpk(v[2], v[3]);
        *reinterpret_cast<u32x2*>(&T[lr * 64 + lc]) = o;
      }
    }
  }
  __syncthreads();
  if (MODE == 1 || MODE == 2) {
#pragma unroll
    for (int i = 0; i < BM / 32; ++i) {
      const int lr = i * 32 + (tid >> 3), c8 = (tid & 7) * 8;
      u32x4 t = *reinterpret_cast<const u32x4*>(&T[lr * 64 + c8]);
      if (MODE == 1) {
        u32x4 rv = *reinterpret_cast<const u32x4*>(resid + (size_t)(m0 + lr) * N + n0 + c8);
        float ps = 0.f, pq = 0.f;
#pragma unroll
        for (int j = 0; j < 4; ++j) {
          float y0 = rbf(__uint_as_float(rv[j] << 16) + __uint_as_float(t[j] << 16));
          float y1 = rbf(__uint_as_float(rv[j] & 0xffff0000u) + __uint_as_float(t[j] & 0xffff0000u));
          ps += y0 + y1; pq += y0 * y0 + y1 * y1;
          t[j] = cvtpk(y0, y1);
        }
        if (stat_out != nullptr) {
          ps += __shfl_xor(ps, 1); pq += __shfl_xor(pq, 1);
          ps += __shfl_xor(ps, 2); pq += __shfl_xor(pq, 2);
          ps += __shfl_xor(ps, 4); pq += __shfl_xor(pq, 4);
          if ((tid & 7) == 0) stat_out[(size_t)(m0 + lr) * 7 + blockIdx.x] = make_float2(ps, pq);
        }
      }
      *reinterpret_cast<u32x4*>(out + (size_t)(m0 + lr) * N + n0 + c8) = t;
    }
  } else if (!vtile) {
    u16* dst = (n0 < DM) ? Qb : Kb;
    const int nb = (n0 < DM) ? n0 : n0 - DM;
#pragma unroll
    for (int i = 0; i < BM / 16; ++i) {
      const int lr = i * 16 + (tid >> 4), c4 = (tid & 15) * 4;
      const int rem = nb + c4, head = rem / DH, d = rem % DH;
      u32x2 t = *reinterpret_cast<const u32x2*>(&T[lr * 64 + c4]);
      u16* p = dst + ((size_t)head * SEQ + m0 + lr) * 32 + d;
      *reinterpret_cast<u32x2*>(p) = t;
      if (d == 24) { u32x2 z = {0u, 0u}; *reinterpret_cast<u32x2*>(p + 4) = z; }
    }
  } else {
#pragma unroll
    for (int i = 0; i < BM / 32; ++i) {
      const int idx = i * 256 + tid, lc = idx / (BM / 8), t8 = (idx % (BM / 8)) * 8;
      const int rem = n0 - 2 * DM + lc, head = rem / DH, d = rem % DH;
      u32x2 a0 = *reinterpret_cast<const u32x2*>(&T[lc * BM + (t8 & ~15) + ((t8 & 8) ? 4 : 0)]);
      u32x2 a1 = *reinterpret_cast<const u32x2*>(&T[lc * BM + (t8 & ~15) + ((t8 & 8) ? 12 : 8)]);
      u32x4 o = {a0[0], a0[1], a1[0], a1[1]};
      u16* p = Vt + ((size_t)head * 32 + d) * SEQ + m0 + t8;
      *reinterpret_cast<u32x4*>(p) = o;
      if (d == 27) {
        const unsigned one2 = 0x3f803f80u;
        u32x4 ones = {one2, one2, one2, one2}, z = {0u, 0u, 0u, 0u};
        *reinterpret_cast<u32x4*>(p + SEQ) = ones; *reinterpret_cast<u32x4*>(p + 2 * SEQ) = z;
        *reinterpret_cast<u32x4*>(p + 3 * SEQ) = z; *reinterpret_cast<u32x4*>(p + 4 * SEQ) = z;
      }
    }
  }
}

__global__ __launch_bounds__(512) void attn_kernel(const u16* __restrict__ Qb, const u16* __restrict__ Kb,
                                                   const u16* __restrict__ Vt, u16* __restrict__ o) {
  __shared__ __attribute__((aligned(16))) u16 sKV[2][2][2][128 * 32];
  const int half = threadIdx.x >> 8;
  u16 (*sK)[128 * 32] = sKV[half][0];
  u16 (*sV)[128 * 32] = sKV[half][1];
  const int tid = threadIdx.x & 255, w = tid >> 6, lane = tid & 63, r = lane & 31, hh = lane >> 5;
  const int head = blockIdx.x >> 4, qblk = blockIdx.x & 15;
  const int q0 = qblk * 128 + w * 32;
  const u16* Kh = Kb + (size_t)head * SEQ * 32;
  const u16* Vh = Vt + (size_t)head * 32 * SEQ;

  bf16x8 qf[2];
#pragma unroll
  for (int ks = 0; ks < 2; ++ks)
    qf[ks] = *reinterpret_cast<const bf16x8*>(Qb + ((size_t)head * SEQ + q0 + r) * 32 + ks * 16 + 8 * hh);

  u32x4 rk[2], rv[2];
  auto gload = [&](int key0) {
#pragma unroll
    for (int i = 0; i < 2; ++i) {
      int ch = tid + 256 * i;
      rk[i] = *reinterpret_cast<const u32x4*>(Kh + (size_t)key0 * 32 + ch * 8);
      int d = ch >> 4, c = ch & 15;
      rv[i] = *reinterpret_cast<const u32x4*>(Vh + (size_t)d * SEQ + key0 + c * 8);
    }
  };
  auto swrite = [&](int buf) {
#pragma unroll
    for (int i = 0; i < 2; ++i) {
      int ch = tid + 256 * i;
      int row = ch >> 2, c = ch & 3;
      *reinterpret_cast<u32x4*>(&sK[buf][row * 32 + ((c ^ ((row >> 2) & 3)) * 8)]) = rk[i];
      int d = ch >> 4, cv = ch & 15;
      *reinterpret_cast<u32x4*>(&sV[buf][d * 128 + ((cv ^ (d & 15)) * 8)]) = rv[i];
    }
  };

  f32x16 oacc, negm;
#pragma unroll
  for (int i = 0; i < 16; ++i) { oacc[i] = 0.f; negm[i] = 0.f; }
  bool first = true;

  const int kbase = half * (SEQ / 2);
  gload(kbase); swrite(0); __syncthreads();
  for (int st = 0; st < SEQ / 256; ++st) {
    const int buf = st & 1;
    if (st + 1 < SEQ / 256) gload(kbase + (st + 1) * 128);
#pragma unroll
    for (int sub = 0; sub < 4; ++sub) {
      bf16x8 kf[2], vf[2];
#pragma unroll
      for (int ks = 0; ks < 2; ++ks) {
        int row = sub * 32 + r, c = ks * 2 + hh;
        kf[ks] = *reinterpret_cast<const bf16x8*>(&sK[buf][row * 32 + ((c ^ ((row >> 2) & 3)) * 8)]);
        int cv = sub * 4 + ks * 2 + hh;
        vf[ks] = *reinterpret_cast<const bf16x8*>(&sV[buf][r * 128 + ((cv ^ (r & 15)) * 8)]);
      }
      f32x16 s = MFMA32(kf[0], qf[0], negm);
      s = MFMA32(kf[1], qf[1], s);
      float tmax = s[0];
#pragma unroll
      for (int i = 1; i < 16; ++i) tmax = fmaxf(tmax, s[i]);
      if (first || __any(tmax > 8.f)) {
        float other = __shfl_xor(tmax, 32);
        float tm = fmaxf(tmax, other);
        float delta = first ? tm : fmaxf(tm, 0.f);
        float f = __builtin_amdgcn_exp2f(-delta);
#pragma unroll
        for (int i = 0; i < 16; ++i) { s[i] -= delta; negm[i] -= delta; oacc[i] *= f; }
        first = false;
      }
      u32x4 p0, p1;
#pragma unroll
      for (int j = 0; j < 4; ++j) {
        p0[j] = cvtpk(__builtin_amdgcn_exp2f(s[2 * j]), __builtin_amdgcn_exp2f(s[2 * j + 1]));
        p1[j] = cvtpk(__builtin_amdgcn_exp2f(s[8 + 2 * j]), __builtin_amdgcn_exp2f(s[8 + 2 * j + 1]));
      }
      oacc = MFMA32(vf[0], __builtin_bit_cast(bf16x8, p0), oacc);
      oacc = MFMA32(vf[1], __builtin_bit_cast(bf16x8, p1), oacc);
    }
    if (st + 1 < SEQ / 256) swrite(buf ^ 1);
    __syncthreads();
  }
  float* mb = reinterpret_cast<float*>(&sKV[0][0][0][0]);
  if (half == 1) {
#pragma unroll
    for (int i = 0; i < 16; ++i) mb[(i * 4 + w) * 64 + lane] = oacc[i];
    mb[(64 + w) * 64 + lane] = negm[0];
  }
  __syncthreads();
  if (half == 1) return;
  {
    const float nm1 = mb[(64 + w) * 64 + lane], nm0 = negm[0];
    const float nm = fminf(nm0, nm1);
    const float f0 = __builtin_amdgcn_exp2f(nm - nm0), f1 = __builtin_amdgcn_exp2f(nm - nm1);
#pragma unroll
    for (int i = 0; i < 16; ++i) oacc[i] = oacc[i] * f0 + mb[(i * 4 + w) * 64 + lane] * f1;
  }
  float l = __shfl(oacc[12], r + 32);
  float inv = 1.f / l;
  u16* op = o + (size_t)(q0 + r) * DM + head * DH;
#pragma unroll
  for (int g = 0; g < 4; ++g) {
    int d = 8 * g + 4 * hh;
    if (d < DH) {
      u32x2 v; v[0] = cvtpk(oacc[4 * g] * inv, oacc[4 * g + 1] * inv); v[1] = cvtpk(oacc[4 * g + 2] * inv, oacc[4 * g + 3] * inv);
      *reinterpret_cast<u32x2*>(op + d) = v;
    }
  }
}

extern "C" void kernel_launch(void* const* d_in, const int* in_sizes, int n_in,
                              void* d_out, int out_size, void* d_ws, size_t ws_size,
                              hipStream_t stream) {
  const u16* x0 = (const u16*)d_in[0];
  const u16* wqkv = (const u16*)d_in[1];
  const u16* wo = (const u16*)d_in[2];
  const u16* wg = (const u16*)d_in[4];
  const u16* wu = (const u16*)d_in[5];
  const u16* wd = (const u16*)d_in[6];
  u16* out = (u16*)d_out;
  char* ws = (char*)d_ws;
  size_t off = 0;
  auto take = [&](size_t bytes) { void* p = ws + off; off += (bytes + 255) & ~(size_t)255; return p; };
  u16* Qb = (u16*)take((size_t)NH * SEQ * 32 * 2);
  u16* Kb = (u16*)take((size_t)NH * SEQ * 32 * 2);
  u16* Vt = (u16*)take((size_t)NH * SEQ * 32 * 2);
  u16* ob = (u16*)take((size_t)SEQ * DM * 2);
  u16* x1 = (u16*)take((size_t)SEQ * DM * 2);
  u16* x2 = (u16*)take((size_t)SEQ * DM * 2);
  u16* hb = (u16*)take((size_t)SEQ * FF * 2);
  float* csum = (float*)take(4928 * 4);
  float2* stat0 = (float2*)take(SEQ * 8);
  float2* statA = (float2*)take(SEQ * 14 * 8);
  float2* statB = (float2*)take(SEQ * 14 * 8);

  prep_kernel<<<77 + 128, 1024, 0, stream>>>(x0, wqkv, wg, wu, csum, stat0);
  const u16* xin = x0;
  for (int layer = 0; layer < 2; ++layer) {
    u16* xo = (layer == 0) ? x2 : out;
    gemm_kernel<64, 0, 3><<<dim3(21, 32), 256, 0, stream>>>(xin, DM, wqkv, nullptr, 3 * DM, nullptr, nullptr, Qb, Kb, Vt, csum,
                                                          layer == 0 ? stat0 : statB, layer == 0 ? 1 : 7, nullptr);
    attn_kernel<<<256, 512, 0, stream>>>(Qb, Kb, Vt, ob);
    gemm_kernel<64, 1, 4><<<dim3(7, 32), 256, 0, stream>>>(ob, DM, wo, nullptr, DM, xin, x1, nullptr, nullptr, nullptr, nullptr, nullptr, 0, statA);
    gemm_kernel<128, 2, 2><<<dim3(28, 16), 256, 0, stream>>>(x1, DM, wg, wu, FF, nullptr, hb, nullptr, nullptr, nullptr, csum, statA, 7, nullptr);
    gemm_kernel<64, 1, 4><<<dim3(7, 32), 256, 0, stream>>>(hb, FF, wd, nullptr, DM, x1, xo, nullptr, nullptr, nullptr, nullptr, nullptr, 0, statB);
    xin = xo;
  }
}
```
